```python
import torch

def _softmax(x, axis=-1):
    return torch.softmax(x, dim=axis)
def _gelu(x):
    return torch.nn.functional.gelu(x, approximate="tanh")
def _silu(x):
    return torch.nn.functional.silu(x)
def _sigmoid(x):
    return torch.sigmoid(x)
def _layernorm(x, axis=-1, eps=1e-5):
    m = x.mean(dim=axis, keepdim=True)
    v = x.var(dim=axis, keepdim=True, unbiased=False)
    return (x - m) / torch.sqrt(v + eps)
def _scatter_add(tgt, idx, src, axis=0):
    return tgt.clone().index_add_(axis, idx, src)
def _segment_sum(src, seg, n):
    out = torch.zeros((n,) + src.shape[1:], dtype=src.dtype)
    return out.index_add_(0, seg, src)
def _pad(x, pw):
    flat = tuple(v for pair in reversed(pw) for v in pair)
    return torch.nn.functional.pad(x, flat)

kernel_name = 'parallel_sum__linear_kernel_hedgehog__dense_activation_silu__prompt'

def setup_inputs():

    _w = (lambda a: (lambda t: t.to(torch.bfloat16) if t.is_floating_point() else t)(torch.as_tensor(a)))
    def _A_setup():
        B, S, D, H = 1, 2048, 512, 32
        Dh = D // H
        return {
            "x": _w((lambda *s: torch.randn(*s, dtype=torch.bfloat16) * 0.05)(B, S, D)),
            "wqkv": _w((lambda *s: torch.randn(*s, dtype=torch.bfloat16) * 0.2)(D, 3 * D)),
            "w_phi": _w((lambda *s: torch.randn(*s, dtype=torch.bfloat16) * 0.2)(H, Dh, Dh) * (Dh ** -0.5)),
            "wo": _w((lambda *s: torch.randn(*s, dtype=torch.bfloat16) * 0.05)(D, D)),
        }
    _A_in = _A_setup()

    def _B_setup():
        B, S, D, F = 1, 2048, 512, 1792
        return {
            "x": _w((lambda *s: torch.randn(*s, dtype=torch.bfloat16) * 0.05)(B, S, D)),
            "w1": _w((lambda *s: torch.randn(*s, dtype=torch.bfloat16) * 0.8)(D, F) * (D ** -0.5)),
            "w2": _w((lambda *s: torch.randn(*s, dtype=torch.bfloat16) * 0.1)(F, D) * (F ** -0.5)),
        }
    _B_in = _B_setup()

    return {
        "A_x": _A_in["x"],
        "A_wqkv": _A_in["wqkv"],
        "A_w_phi": _A_in["w_phi"],
        "A_wo": _A_in["wo"],
        "B_x": _B_in["x"],
        "B_w1": _B_in["w1"],
        "B_w2": _B_in["w2"],
    }

def reference(A_x, A_wqkv, A_w_phi, A_wo, B_x, B_w1, B_w2):
    def _A_ref(x, wqkv, w_phi, wo):
        B, S, D = x.shape
        H = 32; Dh = D // H
        qkv = (lambda x, *d: x.permute(*d))((x @ wqkv).reshape(B, S, 3, H, Dh), 2, 0, 3, 1, 4)
        q, k, v = qkv[0], qkv[1], qkv[2]
        pq = torch.einsum("bhsd,hdf->bhsf", q, w_phi)
        pk = torch.einsum("bhsd,hdf->bhsf", k, w_phi)
        phi_q = torch.concatenate([_softmax(pq, axis=-1), _softmax(-pq, axis=-1)], axis=-1)
        phi_k = torch.concatenate([_softmax(pk, axis=-1), _softmax(-pk, axis=-1)], axis=-1)
        kv = torch.einsum("bhsf,bhsd->bhfd", phi_k, v)
        z  = torch.einsum("bhsf->bhf", phi_k)
        num = torch.einsum("bhqf,bhfd->bhqd", phi_q, kv)
        den = torch.einsum("bhqf,bhf->bhq", phi_q, z)[..., None] + 1e-6
        o = num / den
        return (lambda x, *d: x.permute(*d))(o, 0, 2, 1, 3).reshape(B, S, D) @ wo

    def _B_ref(x, w1, w2):
        h = torch.einsum("bsd,df->bsf", x, w1)
        h = _silu(h)
        out = torch.einsum("bsf,fd->bsd", h, w2)
        return out

    x = A_x
    xn = _layernorm(x, axis=-1)
    return x + _A_ref(xn, A_wqkv, A_w_phi, A_wo) + _B_ref(xn, B_w1, B_w2)
```

```cpp
#if defined(__HIP_DEVICE_COMPILE__)
#pragma clang attribute push(__attribute__((target("no-packed-fp32-ops"))), apply_to = function)
#endif
#include <hip/hip_runtime.h>
#include <stdint.h>
#include <type_traits>

typedef short s16x4 __attribute__((ext_vector_type(4)));
typedef short s16x8 __attribute__((ext_vector_type(8)));
typedef __bf16 bf16x8 __attribute__((ext_vector_type(8)));
typedef __bf16 bf16x2 __attribute__((ext_vector_type(2)));
typedef float f32x4 __attribute__((ext_vector_type(4)));
typedef float f32x2 __attribute__((ext_vector_type(2)));
typedef unsigned u32x4 __attribute__((ext_vector_type(4)));
typedef unsigned u32x2 __attribute__((ext_vector_type(2)));
typedef unsigned long long u64;
#define LAS __attribute__((address_space(3)))
#define GAS __attribute__((address_space(1)))
#define DI __device__ __forceinline__
#define SB() __builtin_amdgcn_sched_barrier(0)

constexpr int LDS_QKV = 49152;
constexpr int LDS_FA = 86016;
constexpr int LDS_WB = 110592;
constexpr int LDS_SCR = 98304;
constexpr int LDS_MISC = 159744;
constexpr int LDS_WPHI = LDS_MISC;
constexpr int LDS_CS = LDS_MISC + 1024;
constexpr int LDS_ST = LDS_MISC + 2048;
constexpr int LDS_FLG = LDS_MISC + 3072;
constexpr int LDS_TOTAL = 163840;

constexpr size_t WS_EPOCH = 0;
constexpr size_t WS_MB = 65536;
constexpr size_t WS_KVP = 524288;
constexpr int KVP_B = 1088;
constexpr int KVP_SP = 1024 * KVP_B;
constexpr int KVP_CP = KVP_SP + 524288;
constexpr int KVP_TOTAL = KVP_CP + 524288;
constexpr size_t WS_HB = 4u << 20;
constexpr size_t HB_BYTES = (size_t)128 * 72 * 1024;
constexpr unsigned SPIN_CAP = 1u << 16;

template <int I, int N, class F> DI void static_for(F&& f) {
  if constexpr (I < N) { f(std::integral_constant<int, I>{}); static_for<I + 1, N>(f); }
}

DI f32x4 mfma32(s16x8 a, s16x8 b, f32x4 c) {
  return __builtin_amdgcn_mfma_f32_16x16x32_bf16(__builtin_bit_cast(bf16x8, a), __builtin_bit_cast(bf16x8, b), c, 0, 0, 0);
}
DI f32x4 mfma16(s16x4 a, s16x4 b, f32x4 c) { return __builtin_amdgcn_mfma_f32_16x16x16bf16_1k(a, b, c, 0, 0, 0); }
DI unsigned pk2(float a, float b) { f32x2 v = {a, b}; return __builtin_bit_cast(unsigned, __builtin_convertvector(v, bf16x2)); }
DI s16x4 pk4(f32x4 v) { u32x2 r = {pk2(v[0], v[1]), pk2(v[2], v[3])}; return __builtin_bit_cast(s16x4, r); }
DI s16x8 cat(s16x4 a, s16x4 b) { return __builtin_shufflevector(a, b, 0, 1, 2, 3, 4, 5, 6, 7); }
DI s16x4 trd(unsigned addr) { return __builtin_amdgcn_ds_read_tr16_b64_v4i16((LAS s16x4*)(size_t)addr); }
DI s16x8 trd2(unsigned addr, int toff) { return cat(trd(addr), trd(addr + toff)); }
DI f32x4 lds_rd4(unsigned addr) { return *(LAS f32x4*)(size_t)addr; }
DI void lds_wr4(unsigned addr, f32x4 v) { *(LAS f32x4*)(size_t)addr = v; }

DI void dma16(unsigned ldsoff, const void* sbase, unsigned voff) {
  asm volatile("s_mov_b32 m0, %0\n\ts_nop 0\n\tglobal_load_lds_dwordx4 %1, %2" ::"s"(ldsoff), "v"(voff), "s"(sbase));
}
DI void touch(unsigned ldsoff, const void* p) {
  asm volatile("s_mov_b32 m0, %0\n\ts_nop 0\n\tglobal_load_lds_dword %1, off" ::"s"(ldsoff), "v"(p));
}
template <int N> DI void wait_vm() { asm volatile("s_waitcnt vmcnt(%0)" ::"n"(N > 63 ? 63 : N) : "memory"); }
template <int N> DI void wait_vm_bar() {
  asm volatile("s_waitcnt vmcnt(%0) lgkmcnt(0)\n\ts_barrier" ::"n"(N > 63 ? 63 : N) : "memory");
}
DI void lds_bar() { asm volatile("s_waitcnt lgkmcnt(0)\n\ts_barrier" ::: "memory"); }

DI float fmx(float a, float b) { return __builtin_amdgcn_fmed3f(a, b, __builtin_inff()); }
DI void sw16(float& a, float& b) {
  auto r = __builtin_amdgcn_permlane16_swap(__float_as_uint(a), __float_as_uint(b), false, false);
  a = __uint_as_float(r[0]); b = __uint_as_float(r[1]);
}
DI void sw32(float& a, float& b) {
  auto r = __builtin_amdgcn_permlane32_swap(__float_as_uint(a), __float_as_uint(b), false, false);
  a = __uint_as_float(r[0]); b = __uint_as_float(r[1]);
}
template <bool MAX> DI void reduce4(float& a, float& b, float& c, float& d) {
  sw16(a, b); float m0 = MAX ? fmx(a, b) : a + b;
  sw16(c, d); float m1 = MAX ? fmx(c, d) : c + d;
  sw32(m0, m1); float n = MAX ? fmx(m0, m1) : m0 + m1;
  float n2 = n; sw32(n, n2);
  a = n; b = n; sw16(a, b);
  c = n2; d = n2; sw16(c, d);
}
constexpr float LOG2E = 1.4426950408889634f;

DI void dual_softmax4(f32x4 (&p)[4], s16x4 (&pp)[4], s16x4 (&pn)[4]) {
  float a[4], b[4];
#pragma unroll
  for (int r = 0; r < 4; ++r) {
    p[r] *= LOG2E;
    a[r] = fmx(fmx(p[r][0], p[r][1]), fmx(p[r][2], p[r][3]));
    b[r] = fmx(fmx(-p[r][0], -p[r][1]), fmx(-p[r][2], -p[r][3]));
  }
  reduce4<true>(a[0], b[0], a[1], b[1]);
  reduce4<true>(a[2], b[2], a[3], b[3]);
  f32x4 e[4], f[4];
  float s[4], t[4];
#pragma unroll
  for (int r = 0; r < 4; ++r) {
#pragma unroll
    for (int i = 0; i < 4; ++i) { e[r][i] = __builtin_amdgcn_exp2f(p[r][i] - a[r]); f[r][i] = __builtin_amdgcn_exp2f(-p[r][i] - b[r]); }
    s[r] = (e[r][0] + e[r][1]) + (e[r][2] + e[r][3]); t[r] = (f[r][0] + f[r][1]) + (f[r][2] + f[r][3]);
  }
  reduce4<false>(s[0], t[0], s[1], t[1]);
  reduce4<false>(s[2], t[2], s[3], t[3]);
#pragma unroll
  for (int r = 0; r < 4; ++r) { pp[r] = pk4(e[r] * __builtin_amdgcn_rcpf(s[r])); pn[r] = pk4(f[r] * __builtin_amdgcn_rcpf(t[r])); }
}
DI s16x4 silu_t(f32x4 t) {
  f32x4 o;
#pragma unroll
  for (int i = 0; i < 4; ++i) o[i] = t[i] * __builtin_amdgcn_rcpf(__builtin_fmaf(__builtin_amdgcn_exp2f(t[i]), -LOG2E, -LOG2E));
  return pk4(o);
}

template <int MASK> DI void poll_flags(const u64* p, u64 tag, int lane) {
  const GAS u64* g = (const GAS u64*)p + (lane & MASK);
#pragma nounroll
  for (unsigned sp = 0; sp < SPIN_CAP; ++sp) {
    u64 v = __hip_atomic_load(g, __ATOMIC_RELAXED, __HIP_MEMORY_SCOPE_AGENT);
    if (__all(v == tag)) break;
    __builtin_amdgcn_s_sleep(1);
  }
  __builtin_amdgcn_fence(__ATOMIC_ACQUIRE, "wavefront");
}
DI void set_flag(u64* p, u64 tag) { __hip_atomic_store((GAS u64*)p, tag, __ATOMIC_RELAXED, __HIP_MEMORY_SCOPE_AGENT); }

#define NPRE 4
constexpr int NT_F = 12, NT_A = 6;
template <int ROLE> constexpr int n_w(int t) { return (t + 3 >= 0 && t + 3 <= 7) ? (ROLE ? 2 : 3) : (ROLE && (t == 5 || t == 6)) ? 6 : 0; }
constexpr int n_xc(int t) { return (t + 3 >= 0 && t + 3 <= 7) ? 2 : 0; }
template <int ROLE> constexpr int n_iter(int t) { return n_w<ROLE>(t) + n_xc(t); }
template <int ROLE> constexpr int n_post(int t) { return ROLE ? (t == 0 ? 1 : 0) : (t == 0 || t == 1) ? 1 : t == 3 ? 4 : t == 5 ? 5 : (t == 6 || t == 7) ? 1 : 0; }
template <int ROLE> constexpr int n_wait(int t) { return (!ROLE && t >= 7) ? 63 : n_post<ROLE>(t - 2) + n_iter<ROLE>(t - 1) + n_post<ROLE>(t - 1); }

#define IC(n) std::integral_constant<int, n>{}
#define F4(v) __builtin_bit_cast(f32x4, v)

DI void lds_flag_set(unsigned addr) {
  asm volatile("s_waitcnt lgkmcnt(0)" ::: "memory");
  *(volatile LAS unsigned*)(size_t)addr = 1u;
}
DI void lds_flag_wait(unsigned addr, unsigned val) {
  for (unsigned sp = 0; sp < (1u << 24); ++sp) {
    if (*(volatile LAS unsigned*)(size_t)addr >= val) break;
  }
  asm volatile("" ::: "memory");
}
DI float row16_sum(float v) {
  v += __builtin_bit_cast(float, __builtin_amdgcn_update_dpp(0, __builtin_bit_cast(int, v), 0xB1, 0xf, 0xf, true));
  v += __builtin_bit_cast(float, __builtin_amdgcn_update_dpp(0, __builtin_bit_cast(int, v), 0x4E, 0xf, 0xf, true));
  v += __builtin_bit_cast(float, __builtin_amdgcn_update_dpp(0, __builtin_bit_cast(int, v), 0x141, 0xf, 0xf, true));
  v += __builtin_bit_cast(float, __builtin_amdgcn_update_dpp(0, __builtin_bit_cast(int, v), 0x140, 0xf, 0xf, true));
  return v;
}
DI f32x2 hsel(bool c, f32x4 a) { f32x2 r = {c ? a[2] : a[0], c ? a[3] : a[1]}; asm("" : "+v"(r)); return r; }
DI f32x2 vsel(bool c, f32x2 a, f32x2 b) { f32x2 r = {c ? a[0] : b[0], c ? a[1] : b[1]}; asm("" : "+v"(r)); return r; }
DI int swap23(int v) { return (v & ~12) | ((v & 4) << 1) | ((v & 8) >> 1); }
DI void ald8(u64& d, const void* p) { asm volatile("global_load_dwordx2 %0, %1, off sc1" : "=v"(d) : "v"(p)); }
DI void rel16(u32x4& d, const void* sb, unsigned vo) { asm volatile("global_load_dwordx4 %0, %1, %2 sc1" : "+v"(d) : "v"(vo), "s"(sb)); }
DI void ald16(u32x4& d, const void* sb, unsigned vo) { asm volatile("global_load_dwordx4 %0, %1, %2 sc1" : "=v"(d) : "v"(vo), "s"(sb)); }

__global__ __launch_bounds__(512, 2) void fused_kernel(const char* __restrict__ x, const char* __restrict__ wqkv, const char* __restrict__ wphi,
                                                       const char* __restrict__ wo, const char* __restrict__ w1, const char* __restrict__ w2,
                                                       char* __restrict__ out, char* __restrict__ ws) {
  extern __shared__ __attribute__((aligned(16))) char smem[];
  const int tid = (int)__builtin_amdgcn_workitem_id_x(), lane = tid & 63;
  const int w = __builtin_amdgcn_readfirstlane(tid >> 6);
  const int rg = w & 1, sel = (w >> 1) & 1, grp = w & 3;
  const int bid = (int)__builtin_amdgcn_workgroup_id_x(), xcd = bid & 7, idx = bid >> 3;
  const int r = (xcd & 1) * 8 + (idx & 7), c = (xcd >> 1) * 4 + (idx >> 3);
  const int mt = xcd * 4 + (idx & 3), nt = idx >> 2;
  const int odd = c & 1;
  const int rot = (r + c) & 7;
  const int l15 = lane & 15, g = lane >> 4, q4 = (lane & 15) >> 2, p4 = lane & 3;
  const unsigned lbase = (unsigned)(size_t)(LAS char*)smem;
  u64 epv;
  ald8(epv, ws + WS_EPOCH);
  if (w < 2) {
    const char* p = w == 0 ? w2 + (size_t)(bid * 56 + (lane < 56 ? lane : 0)) * 128 : wo + (size_t)(bid * 16 + l15) * 128;
    touch(lbase + LDS_ST + w * 256, p);
  }
  if (w == 2 || w == 3) {
    u64 pc; unsigned len;
    asm volatile("s_getpc_b64 %0\n.Lfk_pc0:\n\ts_mov_b32 %1, .Lfk_end-.Lfk_pc0" : "=s"(pc), "=s"(len));
    const unsigned o = (unsigned)(((idx & 3) * 128 + (w - 2) * 64 + lane) * 128);
    touch(lbase + LDS_ST + w * 256, (const char*)pc + (o < len ? o : 0u));
  }
  const int y = lane >> 3, c8 = lane & 7;
  const unsigned vx0 = (unsigned)(y * 1024 + ((c8 ^ (y >> 1)) * 16));
  const unsigned vx1 = (unsigned)((8 + y) * 1024 + ((c8 ^ (4 + (y >> 1))) * 16));
  const char* xw = x + (size_t)(r * 128 + w * 16) * 1024;
  auto issue_x = [&](auto UU, int half) {
    constexpr int u = UU;
    dma16(lbase + (u % 3) * 16384 + w * 2048 + half * 1024, xw + ((u + rot) & 7) * 128, half ? vx1 : vx0);
  };
  issue_x(IC(0), 0); issue_x(IC(0), 1);

  const int rho = 16 * (g >> 1) + 4 * (g & 1) + q4;
  const int swh = 2 * (g & 1) + (q4 >> 1);
  const int pin = (p4 >> 1) * 16 + (p4 & 1) * 8;
  const int ch8 = c8 ^ (((lane >> 4) & 3) << 1);
  const int xsw = (l15 >> 1) & 7;
  const int rowA = rg * 64 + sel * 32, rowB = rg * 64 + (1 - sel) * 32;
  const int a_col = odd ? 112 * c + 48 : 112 * c, b_col = odd ? 112 * c : 112 * c + 64;
  auto slot_of = [&](int ww, int j) -> unsigned {
    const int ab = j < 2 ? 65536 + j * 16384 : LDS_WB + (j - 1) * 16384;
    const int fb = j < 4 ? j * 16384 : LDS_WB;
    return lbase + (ww >= 4 ? ab : fb) + (ww & 3) * 4096;
  };
  const s16x8 ones = {0x3F80, 0x3F80, 0x3F80, 0x3F80, 0x3F80, 0x3F80, 0x3F80, 0x3F80};
  const f32x4 zero4 = {0.f, 0.f, 0.f, 0.f};
  f32x4 acc[4][4];
  u64 tag;

  auto role_body = [&](auto RR) {
    constexpr int ROLE = RR;
    constexpr int NT = ROLE ? NT_A : NT_F, NP = 4;
    unsigned vw[3];
    if constexpr (ROLE == 0) {
#pragma unroll
      for (int j = 0; j < 3; ++j) {
        const int n = (grp * 3 + j) * 64 + lane, R = n / 12, ch = (n % 12) ^ (((R >> 2) & 1) << 1);
        vw[j] = (unsigned)(swap23(R) * 3072 + (ch >> 2) * 1024 + 64 * c + (ch & 3) * 16);
      }
    } else {
      vw[0] = (unsigned)((8 * (y >> 2) + (y & 3)) * 3584 + a_col * 2 + ch8 * 16); vw[1] = 0; vw[2] = 0;
    }
    auto issue_wb = [&](int s, int j) {
      const int n = j * 64 + lane;
      dma16(lbase + LDS_WB + s * 3072 + j * 1024, w1 + (size_t)(((s + 2 * rot) & 15) * 32 * 3584),
            (unsigned)(swap23(n / 6) * 3584 + b_col * 2 + (n % 6) * 16));
    };
    auto issue_w = [&](auto TT, auto KK) {
      constexpr int t = TT, k = KK, s = t + 3;
      if constexpr (t >= 5) issue_wb(4 * grp + 2 * (t - 5) + k / 3, k % 3);
      else if constexpr (ROLE) {
        const int p = 2 * grp + k;
        dma16(lbase + LDS_FA + (s % 3) * 8192 + p * 1024, w1 + (size_t)((((s + rot) & 7) * 64 + 32 * (p >> 2) + 16 * ((p >> 1) & 1) + 4 * (p & 1)) * 3584), vw[0]);
      } else dma16(lbase + LDS_QKV + (s % 3) * 12288 + (grp * 3 + k) * 1024, wqkv + (size_t)(((s + rot) & 7) * 64 * 3072), vw[k]);
    };
    auto dma_iter = [&](auto TT) {
      constexpr int t = TT;
      static_for<0, n_w<ROLE>(t)>([&](auto KK) { issue_w(TT, KK); });
      if constexpr (n_xc(t)) { issue_x(IC(t + 3), 0); issue_x(IC(t + 3), 1); }
    };
    static_for<0, n_w<ROLE>(-3)>([&](auto KK) { issue_w(IC(-3), KK); });
    if (grp < 3) issue_wb(ROLE, grp);
    __builtin_amdgcn_s_barrier();
    dma_iter(IC(-2));
    if constexpr (ROLE == 0) { if (tid < 16) *(LAS unsigned*)(size_t)(lbase + LDS_FLG + tid * 4) = 0u; }
    else { if (w == 6) dma16(lbase + LDS_WPHI, wphi + c * 1024, (unsigned)lane * 16); }

    unsigned axA[2], axB[2];
#pragma unroll
    for (int i = 0; i < 2; ++i) {
      axA[i] = lbase + (rowA + l15) * 128 + (((4 * i + g) ^ xsw) << 4);
      axB[i] = lbase + (rowB + l15) * 128 + (((4 * i + g) ^ xsw) << 4);
    }
    const __amdgpu_buffer_rsrc_t rs_hb = __builtin_amdgcn_make_buffer_rsrc(ws + WS_HB, 0, (int)HB_BYTES, 0x00020000);
    const __amdgpu_buffer_rsrc_t rs_kv = __builtin_amdgcn_make_buffer_rsrc(ws + WS_KVP, 0, KVP_TOTAL, 0x00020000);
    auto rbo = [&](int i) -> int { return ((r * 8 + (((i < 2 ? rowA : rowB) >> 4) + (i & 1))) * 72) * 1024 + lane * 16; };
    auto signal = [&](int kind) {
      if (lane < 8) set_flag((u64*)(ws + WS_MB) + ((((kind * 32 + r * 2 + rg) * 8 + lane) * 4 + (c >> 2)) * 16 + (c & 3) * 2 + sel), tag);
    };
    auto await = [&](int kind) { poll_flags<7>((const u64*)(ws + WS_MB) + (((kind * 32 + mt) * 8 + nt) * 4 + grp) * 16, tag, lane); };

    const unsigned vb2 = (unsigned)(y * 1024 + nt * 128 + ch8 * 16);
    const int rot2 = nt + 2 * (mt & 3);
    auto kstep_of = [&](int T) -> int {
      if (!ROLE) {
        if (T < 8) { const int u = (T + rot2) & 7; return 14 * grp + u + (u >= 6 ? 6 : u >= 2 ? 3 : 0); }
        const int u = (T + rot2) & 3; return 14 * grp + 2 + u + (u == 3 ? 4 : 0);
      }
      return T < 2 ? 14 * grp + 10 + ((T + rot2) & 1) : 56 + 4 * grp + ((T + rot2) & 3);
    };
    auto b2_dma = [&](int T, int slot, int j) {
      const char* src = (ROLE && T >= 2) ? wo + (size_t)(32 * (kstep_of(T) - 56)) * 1024 : w2 + (size_t)(32 * kstep_of(T)) * 1024;
      dma16(slot_of(ROLE * 4 + grp, slot) + j * 1024, src + j * 8192, vb2);
    };
    auto b2_tile = [&](int T, int slot) {
#pragma unroll
      for (int j = 0; j < 4; ++j) b2_dma(T, slot, j);
    };
    s16x8 b2r[NP][4];
    auto b2_frag = [&](int slot, int b) -> s16x8 {
      return trd2(slot_of(ROLE * 4 + grp, slot) + (4 * g + q4) * 128 + ((b ^ swh) << 5) + pin, 2048);
    };
    auto park = [&](int T, int slot) {
#pragma unroll
      for (int b = 0; b < 4; ++b) b2r[T][b] = b2_frag(slot, b);
    };
    auto touch_b2 = [&](int n) {
      const int row = idx * 72 + n * 6 + (lane < 48 ? y : 0);
      touch(lbase + LDS_FLG + 256, (row < 1792 ? w2 + (size_t)row * 1024 : wo + (size_t)(row - 1792) * 1024) + c8 * 128);
    };
    float rstd[4], nmr[4];
    auto load_stats = [&]() {
#pragma unroll
      for (int i = 0; i < 4; ++i) {
        const f32x2 so = *(LAS f32x2*)(size_t)(lbase + LDS_ST + ((i < 2 ? rowA : rowB) + (i & 1) * 16 + l15) * 8);
        rstd[i] = so[0]; nmr[i] = so[1];
      }
    };
    auto get_tag = [&]() {
      asm volatile("" : "+v"(epv));
      const unsigned lo = __builtin_amdgcn_readfirstlane((unsigned)epv), hi = __builtin_amdgcn_readfirstlane((unsigned)(epv >> 32));
      tag = (((u64)hi << 32) | lo) + 1;
    };

    if constexpr (ROLE == 0) {
      const unsigned aa0 = lbase + LDS_FA + rho * 128 + (((2 * sel) ^ swh) << 5) + pin, aa1 = lbase + LDS_FA + rho * 128 + (((2 * sel + 1) ^ swh) << 5) + pin;
      const unsigned abp = lbase + LDS_WB + rho * 96 + pin + (odd ? 32 : 0), abs_ = lbase + LDS_WB + rho * 96 + pin + (odd ? 0 : 64);
      s16x8 xk[16][2], xo[2][2][2], bf[2][2][2];
      f32x4 a[8], cs[5], rs[2], gram[2];
#pragma unroll
      for (int i = 0; i < 8; ++i) a[i] = zero4;
#pragma unroll
      for (int i = 0; i < 5; ++i) cs[i] = zero4;
#pragma unroll
      for (int i = 0; i < 2; ++i) { rs[i] = zero4; gram[i] = zero4; }
      auto rd = [&](auto SS, auto KK) {
        constexpr int s = SS, u = s & 1, h = KK / 6, k = KK % 6;
        constexpr int xof = (s % 3) * 16384, so = (s % 3) * 8192 + h * 4096;
        if constexpr (k == 0) xk[2 * s + h][0] = *(LAS s16x8*)(size_t)(axA[h] + xof);
        else if constexpr (k == 1) bf[u][h][0] = trd2(aa0 + so, 1024);
        else if constexpr (k == 2) bf[u][h][1] = trd2(aa1 + so, 1024);
        else if constexpr (k == 3) xk[2 * s + h][1] = *(LAS s16x8*)(size_t)(axA[h] + xof + 2048);
        else if constexpr (k == 4) xo[u][h][0] = *(LAS s16x8*)(size_t)(axB[h] + xof);
        else xo[u][h][1] = *(LAS s16x8*)(size_t)(axB[h] + xof + 2048);
      };
      auto mm = [&](auto TT, auto KK) {
        constexpr int t = TT, u = t & 1, h = KK / 8, i = (KK % 8) / 2, j = KK % 2;
        if constexpr (i < 2) a[4 * j + i] = mfma32(bf[u][h][j], xk[2 * t + h][i], a[4 * j + i]);
        else a[4 * j + i] = mfma32(bf[u][h][j], xo[u][h][i - 2], a[4 * j + i]);
      };
      const char* gs = ws + WS_KVP + KVP_SP + (r * 16 + (c & 8)) * 2048, *gc = ws + WS_KVP + KVP_CP + (c * 16 + (r & 8)) * 2048;
      const unsigned gvo = (unsigned)((lane >> 5) * 2048 + grp * 512 + (lane & 31) * 16);
      u32x4 gv[4];
      auto g_sum = [&](const char* base, int o, bool live, float& v0, float& v1) {
        const unsigned tg = (unsigned)tag;
#pragma nounroll
        for (unsigned sp = 0; sp < SPIN_CAP; ++sp) {
          unsigned bad = 0;
#pragma unroll
          for (int j = 0; j < 4; ++j) bad |= (gv[o + j][1] ^ tg) | (gv[o + j][3] ^ tg);
          if (__all(bad == 0 || !live)) break;
#pragma unroll
          for (int j = 0; j < 4; ++j) rel16(gv[o + j], base + j * 4096, gvo);
          asm volatile("s_waitcnt vmcnt(0)" : "+v"(gv[o]), "+v"(gv[o + 1]), "+v"(gv[o + 2]), "+v"(gv[o + 3]) :: "memory");
        }
        v0 = (__uint_as_float(gv[o][0]) + __uint_as_float(gv[o + 1][0])) + (__uint_as_float(gv[o + 2][0]) + __uint_as_float(gv[o + 3][0]));
        v1 = (__uint_as_float(gv[o][2]) + __uint_as_float(gv[o + 1][2])) + (__uint_as_float(gv[o + 2][2]) + __uint_as_float(gv[o + 3][2]));
        sw32(v0, v1);
        v0 += v1;
        v1 = v0; sw32(v0, v1);
      };
      static_for<-1, 8>([&](auto TT) {
        constexpr int t = TT;
        wait_vm_bar<n_wait<0>(t)>();
        if constexpr (t == -1) {
          static_for<0, 12>([&](auto KK) { rd(IC(0), KK); });
          s16x8 wb[2][3];
#pragma unroll
          for (int h = 0; h < 2; ++h) { wb[h][0] = trd2(abp + h * 3072, 768); wb[h][1] = trd2(abp + h * 3072 + 32, 768); wb[h][2] = trd2(abs_ + h * 3072, 768); }
          dma_iter(TT); get_tag();
#pragma unroll
          for (int h = 0; h < 2; ++h) {
#pragma unroll
            for (int i = 0; i < 2; ++i) {
              cs[i] = mfma32(bf[0][h][i], ones, cs[i]);
              rs[i] = mfma32(ones, xk[h][i], rs[i]); gram[i] = mfma32(xk[h][i], xk[h][i], gram[i]);
            }
#pragma unroll
            for (int j = 0; j < 3; ++j) cs[2 + j] = mfma32(wb[h][j], ones, cs[2 + j]);
          }
#pragma unroll
          for (int i = 0; i < 5; ++i) asm volatile("" : "+v"(cs[i]));
          asm volatile("" : "+v"(rs[0]), "+v"(rs[1]), "+v"(gram[0]), "+v"(gram[1]));
        }
        else {
          static_for<0, 16>([&](auto KK) {
            constexpr int k = KK;
            mm(TT, KK);
            if constexpr (t + 1 <= 7 && k < 12) rd(IC(t + 1), KK);
            SB();
          });
#pragma unroll
          for (int i = 0; i < 8; ++i) asm volatile("" : "+v"(a[i]));
          dma_iter(TT);
          asm volatile("" ::: "memory");
          if constexpr (t == 0) {
            float d0 = (p4 == 0) ? gram[0][0] : (p4 == 1) ? gram[0][1] : (p4 == 2) ? gram[0][2] : gram[0][3];
            float d1 = (p4 == 0) ? gram[1][0] : (p4 == 1) ? gram[1][1] : (p4 == 2) ? gram[1][2] : gram[1][3];
            d0 = (g == q4) ? d0 : 0.f; d1 = (g == q4) ? d1 : 0.f;
            sw16(d0, d1); d0 += d1;
            d1 = d0; sw32(d0, d1); d0 += d1;
            float r0 = rs[0][0], r1 = rs[1][0];
            asm("" : "+v"(r0), "+v"(r1));
            const u32x4 sp = {__float_as_uint(g & 1 ? r1 : r0), (unsigned)tag, __float_as_uint(d0), (unsigned)tag};
            __builtin_amdgcn_raw_buffer_store_b128(sp, rs_kv, g < 2 ? KVP_SP + (r * 16 + c) * 2048 + (rowA + lane) * 16 : 0x7ffffff0, 0, 16);
          }
          if constexpr (t == 1) {
            const int b = l15 >> 1;
            const bool hh = l15 & 1;
            const f32x2 cv = vsel(b & 4, hsel(hh, cs[4]), vsel(b & 2, vsel(b & 1, hsel(hh, cs[3]), hsel(hh, cs[2])), vsel(b & 1, hsel(hh, cs[1]), hsel(hh, cs[0]))));
            const u32x4 cg = {__float_as_uint(cv[0]), (unsigned)tag, __float_as_uint(cv[1]), (unsigned)tag};
            const int blk = b < 2 ? 6 + 2 * sel + b : 8 + b;
            __builtin_amdgcn_raw_buffer_store_b128(cg, rs_kv, b < (rg ? 0 : sel ? 2 : 5) ? KVP_CP + (c * 16 + r) * 2048 + blk * 128 + g * 32 + (l15 & 1) * 16 : 0x7ffffff0, 0, 16);
          }
          if constexpr (t == 3) {
#pragma unroll
            for (int j = 0; j < 4; ++j) ald16(gv[j], gs + j * 4096, gvo);
          }
          if constexpr (t == 5) {
            asm volatile("s_waitcnt vmcnt(%4)" : "+v"(gv[0]), "+v"(gv[1]), "+v"(gv[2]), "+v"(gv[3]) : "n"(n_iter<0>(4) + n_iter<0>(5)) : "memory");
            float sm, sq;
            g_sum(gs, 0, true, sm, sq);
#pragma unroll
            for (int j = 0; j < 4; ++j) ald16(gv[j], gc + j * 4096, gvo);
            touch_b2(grp * 3);
            const float mean = sm * (1.0f / 512.0f);
            const float var = fmaxf(sq * (1.0f / 512.0f) - mean * mean, 0.f);
            const float rsd = __builtin_amdgcn_rsqf(var + 1e-5f);
            if (lane < 32) *(LAS f32x2*)(size_t)(lbase + LDS_ST + (grp * 32 + lane) * 8) = f32x2{rsd, -mean * rsd};
          }
          if constexpr (t == 6) touch_b2(grp * 3 + 1);
          if constexpr (t == 7) {
            touch_b2(grp * 3 + 2);
            asm volatile("s_waitcnt vmcnt(%4)" : "+v"(gv[0]), "+v"(gv[1]), "+v"(gv[2]), "+v"(gv[3]) : "n"(3) : "memory");
            float c0, c1;
            g_sum(gc, 0, grp * 32 + (lane & 31) < 104, c0, c1);
            if (lane < 32) *(LAS f32x2*)(size_t)(lbase + LDS_CS + (grp * 32 + lane) * 8) = f32x2{c0, c1};
          }
        }
        SB();
      });
      wait_vm_bar<63>();

      load_stats();
      const unsigned cf = lbase + LDS_CS + g * 16;
      const f32x4 ch0 = lds_rd4(cf + (6 + 2 * sel) * 64), ch1 = lds_rd4(cf + (7 + 2 * sel) * 64);
      s16x8 bb[2][3];
      auto rdb = [&](auto SS) {
        constexpr int s = SS;
        bb[s & 1][0] = trd2(abp + s * 3072, 768); bb[s & 1][1] = trd2(abp + s * 3072 + 32, 768); bb[s & 1][2] = trd2(abs_ + s * 3072, 768);
      };
      rdb(IC(0));
#pragma unroll
      for (int i = 0; i < 4; ++i) { rstd[i] *= -LOG2E; nmr[i] *= -LOG2E; }
#pragma unroll
      for (int i = 0; i < 4; ++i) {
        const s16x8 h = cat(silu_t(a[i] * rstd[i] + ch0 * nmr[i]), silu_t(a[4 + i] * rstd[i] + ch1 * nmr[i]));
        __builtin_amdgcn_raw_buffer_store_b128(__builtin_bit_cast(u32x4, h), rs_hb, rbo(i) + ((a_col >> 5) + sel) * 1024, 0, 16);
        SB();
      }
      asm volatile("" ::: "memory");
      f32x4 b[6];
#pragma unroll
      for (int i = 0; i < 6; ++i) b[i] = zero4;
      static_for<0, 16>([&](auto SS) {
        constexpr int s = SS;
        if constexpr (s + 1 < 16) rdb(IC(s + 1));
        b2_dma(s >> 2, s >> 2, s & 3);
        SB();
#pragma unroll
        for (int j = 0; j < 3; ++j)
#pragma unroll
          for (int i = 0; i < 2; ++i) b[2 * j + i] = mfma32(bb[s & 1][j], xk[s][i], b[2 * j + i]);
        SB();
      });
      if (lane == 0) __hip_atomic_fetch_add((LAS unsigned*)(size_t)(lbase + LDS_FLG + 32), 1u, __ATOMIC_RELAXED, __HIP_MEMORY_SCOPE_WORKGROUP);
      {
        const f32x4 cp0 = lds_rd4(cf + 10 * 64), cp1 = lds_rd4(cf + 11 * 64), csg = lds_rd4(cf + 12 * 64);
        const int kp = (b_col + (odd ? 16 : 0)) >> 5, ksg = (b_col + (odd ? 0 : 32)) >> 5;
        const s16x8 h0 = cat(silu_t(b[0] * rstd[0] + cp0 * nmr[0]), silu_t(b[2] * rstd[0] + cp1 * nmr[0]));
        const s16x4 g0 = silu_t(b[4] * rstd[0] + csg * nmr[0]);
        SB();
        wait_vm<16>();
        signal(0);
        __builtin_amdgcn_raw_buffer_store_b128(__builtin_bit_cast(u32x4, h0), rs_hb, rbo(0) + kp * 1024, 0, 16);
        SB();
        const s16x8 h1 = cat(silu_t(b[1] * rstd[1] + cp0 * nmr[1]), silu_t(b[3] * rstd[1] + cp1 * nmr[1]));
        const s16x4 g1 = silu_t(b[5] * rstd[1] + csg * nmr[1]);
        __builtin_amdgcn_raw_buffer_store_b128(__builtin_bit_cast(u32x4, h1), rs_hb, rbo(1) + kp * 1024, 0, 16);
        __builtin_amdgcn_raw_buffer_store_b128(__builtin_bit_cast(u32x4, cat(g0, g1)), rs_hb, rbo(0) + odd * 72 * 1024 + ksg * 1024, 0, 16);
      }
      asm volatile("" ::: "memory");
      wait_vm<3>();
#pragma unroll
      for (int T = 0; T < 4; ++T) park(T, T);
      wait_vm<0>();
      signal(1);
      lds_flag_wait(lbase + LDS_FLG + 32, 4);
      asm volatile("s_waitcnt lgkmcnt(0)" ::: "memory");
    } else {
      const unsigned aq = lbase + LDS_QKV + rho * 192 + pin + ((sel ^ (g & 1)) << 5);
      s16x8 xf[2][2][4], bf[2][2][3];
      f32x4 a[12], cs[3];
#pragma unroll
      for (int i = 0; i < 12; ++i) a[i] = zero4;
#pragma unroll
      for (int i = 0; i < 3; ++i) cs[i] = zero4;
      auto rd = [&](auto SS, auto KK) {
        constexpr int s = SS, u = s & 1, h = KK / 7, k = KK % 7;
        constexpr int xof = (s % 3) * 16384, so = (s % 3) * 12288 + h * 6144;
        if constexpr (k == 0) xf[u][h][0] = *(LAS s16x8*)(size_t)(axA[h] + xof);
        else if constexpr (k <= 3) bf[u][h][k - 1] = trd2(aq + so + (k - 1) * 64, 1536);
        else if constexpr (k == 4) xf[u][h][1] = *(LAS s16x8*)(size_t)(axA[h] + xof + 2048);
        else if constexpr (k == 5) xf[u][h][2] = *(LAS s16x8*)(size_t)(axB[h] + xof);
        else xf[u][h][3] = *(LAS s16x8*)(size_t)(axB[h] + xof + 2048);
      };
      auto mm = [&](auto TT, auto KK) {
        constexpr int u = TT & 1, h = KK / 12, i = (KK % 12) / 3, j = KK % 3;
        if constexpr (j < 2) a[4 * j + i] = mfma32(bf[u][h][j], xf[u][h][i], a[4 * j + i]);
        else a[8 + i] = mfma32(xf[u][h][i], bf[u][h][2], a[8 + i]);
      };
      const int kvb = ((c * 2 + sel) * 2 + rg) * 16;
      const unsigned sme = lbase + LDS_SCR + grp * 2048, spa = lbase + LDS_SCR + (grp ^ 1) * 2048;
      const s16x4 ident = {(short)(4 * g + 0 == l15 ? 0x3F80 : 0), (short)(4 * g + 1 == l15 ? 0x3F80 : 0),
                           (short)(4 * g + 2 == l15 ? 0x3F80 : 0), (short)(4 * g + 3 == l15 ? 0x3F80 : 0)};
      static_for<-1, 8>([&](auto TT) {
        constexpr int t = TT;
        wait_vm_bar<n_wait<1>(t)>();
        if constexpr (t == -1) {
          static_for<0, 14>([&](auto KK) { rd(IC(0), KK); });
          dma_iter(TT); get_tag();
#pragma unroll
          for (int h = 0; h < 2; ++h)
#pragma unroll
            for (int b = 0; b < 3; ++b) cs[b] = mfma32(bf[0][h][b], ones, cs[b]);
          asm volatile("" : "+v"(cs[0]), "+v"(cs[1]), "+v"(cs[2]));
        }
        else {
          dma_iter(TT);
          if constexpr (t == 0) {
            asm volatile("" ::: "memory");
            const int b = l15 >> 1;
            const bool hh = l15 & 1;
            const f32x2 cv = vsel(b & 2, hsel(hh, cs[2]), vsel(b & 1, hsel(hh, cs[1]), hsel(hh, cs[0])));
            const u32x4 cg = {__float_as_uint(cv[0]), (unsigned)tag, __float_as_uint(cv[1]), (unsigned)tag};
            __builtin_amdgcn_raw_buffer_store_b128(cg, rs_kv, b < (rg ? 0 : 3) ? KVP_CP + (c * 16 + r) * 2048 + (2 * b + sel) * 128 + g * 32 + (l15 & 1) * 16 : 0x7ffffff0, 0, 16);
          }
          if constexpr (t + 1 <= 7) static_for<0, NPRE>([&](auto KK) { rd(IC(t + 1), KK); });
          SB();
          static_for<0, 24>([&](auto KK) {
            constexpr int k = KK;
            mm(TT, KK);
            if constexpr (t + 1 <= 7 && (k & 1) == 0 && NPRE + k / 2 < 14) rd(IC(t + 1), IC(NPRE + k / 2));
            SB();
          });
#pragma unroll
          for (int i = 0; i < 12; ++i) asm volatile("" : "+v"(a[i]));
        }
        SB();
      });
      wait_vm_bar<0>();
      const unsigned tag32 = (unsigned)tag;
      load_stats();
      const s16x4 wf = trd(lbase + LDS_WPHI + sel * 512 + lane * 8);
      const f32x4 csq = lds_rd4(lbase + LDS_CS + sel * 64 + g * 16);
      {
        const f32x4 csk = lds_rd4(lbase + LDS_CS + (2 + sel) * 64 + g * 16);
        const float csv = *(LAS float*)(size_t)(lbase + LDS_CS + (4 + sel) * 64 + l15 * 4);
        f32x4 sv[4][2];
#pragma unroll
        for (int i = 0; i < 4; ++i) {
          const unsigned sa = lbase + LDS_ST + ((i < 2 ? rowA : rowB) + (i & 1) * 16 + 4 * g) * 8;
          sv[i][0] = lds_rd4(sa); sv[i][1] = lds_rd4(sa + 16);
        }
        b2_tile(0, 0);
        SB();
        f32x4 pk[4];
#pragma unroll
        for (int i = 0; i < 4; ++i) pk[i] = mfma16(wf, pk4(a[4 + i] * rstd[i] + csk * nmr[i]), zero4);
        s16x4 vb[4];
#pragma unroll
        for (int i = 0; i < 4; ++i) {
          f32x4 vv;
          vv[0] = a[8 + i][0] * sv[i][0][0] + csv * sv[i][0][1];
          vv[1] = a[8 + i][1] * sv[i][0][2] + csv * sv[i][0][3];
          vv[2] = a[8 + i][2] * sv[i][1][0] + csv * sv[i][1][1];
          vv[3] = a[8 + i][3] * sv[i][1][2] + csv * sv[i][1][3];
          vb[i] = pk4(vv);
        }
        SB();
        b2_tile(1, 1);
        SB();
        f32x4 kvp = zero4, kvn = zero4, zp = zero4, zn = zero4;
        s16x4 pp[4], pn[4];
        dual_softmax4(pk, pp, pn);
        SB();
#pragma unroll
        for (int pr = 0; pr < 2; ++pr) {
          const s16x8 ap = cat(pk4(mfma16(pp[2 * pr], ident, zero4)), pk4(mfma16(pp[2 * pr + 1], ident, zero4)));
          const s16x8 an = cat(pk4(mfma16(pn[2 * pr], ident, zero4)), pk4(mfma16(pn[2 * pr + 1], ident, zero4)));
          const s16x8 v8 = cat(vb[2 * pr], vb[2 * pr + 1]);
          kvp = mfma32(ap, v8, kvp); kvn = mfma32(an, v8, kvn);
          zp = mfma32(ap, ones, zp); zn = mfma32(an, ones, zn);
        }
        lds_wr4(sme + lane * 16, rg ? kvp : kvn);
        lds_wr4(sme + 1024 + lane * 16, rg ? zp : zn);
        lds_flag_set(lbase + LDS_FLG + grp * 4);
        lds_flag_wait(lbase + LDS_FLG + (grp ^ 1) * 4, 1);
        const f32x4 kk = (rg ? kvn : kvp) + lds_rd4(spa + lane * 16);
        const f32x4 zz = (rg ? zn : zp) + lds_rd4(spa + 1024 + lane * 16);
        const u32x4 gk = {pk2(kk[0], kk[1]), tag32, pk2(kk[2], kk[3]), tag32};
        const u32x4 gz = {pk2(zz[0], zz[1]), tag32, pk2(zz[2], zz[3]), tag32};
        __builtin_amdgcn_raw_buffer_store_b128(gk, rs_kv, (kvb + r) * KVP_B + lane * 16, 0, 16);
        __builtin_amdgcn_raw_buffer_store_b128(gz, rs_kv, l15 == 0 ? (kvb + r) * KVP_B + 1024 + g * 16 : 0x7ffffff0, 0, 16);
      }
      asm volatile("" ::: "memory");
      wait_vm<2>();
      park(0, 0); park(1, 1);
      asm volatile("s_waitcnt lgkmcnt(0)" ::: "memory");
      b2_tile(2, 0); b2_tile(3, 1);
      s16x8 phq[4];
      {
        f32x4 pq[4];
#pragma unroll
        for (int i = 0; i < 4; ++i) pq[i] = mfma16(wf, pk4(a[i] * rstd[i] + csq * nmr[i]), zero4);
        s16x4 pp[4], pn[4];
        dual_softmax4(pq, pp, pn);
#pragma unroll
        for (int i = 0; i < 4; ++i) { phq[i] = cat(pp[i], pn[i]); asm volatile("" : "+v"(phq[i])); }
      }
      SB();
      __builtin_amdgcn_s_setprio(2);
      f32x4 s = zero4, z;
      {
        u32x4 ld[17];
        const char* kb = ws + WS_KVP + kvb * KVP_B;
        const unsigned zo = (unsigned)(l15 * KVP_B + 1024 + g * 16);
#pragma unroll
        for (int j = 0; j < 16; ++j) ald16(ld[j], kb, (unsigned)(lane * 16 + j * KVP_B));
        ald16(ld[16], kb, zo);
        wait_vm<17>();
        park(2, 0); park(3, 1);
        asm volatile("s_waitcnt lgkmcnt(0)" ::: "memory");
        wait_vm<0>();
#pragma nounroll
        for (unsigned sp = 0; sp < SPIN_CAP; ++sp) {
          unsigned bad = 0;
#pragma unroll
          for (int j = 0; j < 17; ++j) { asm volatile("" : "+v"(ld[j])); bad |= (ld[j][1] ^ tag32) | (ld[j][3] ^ tag32); }
          if (__all(bad == 0)) break;
#pragma nounroll
          for (; sp < SPIN_CAP && !__all(((ld[16][1] ^ tag32) | (ld[16][3] ^ tag32)) == 0); ++sp) {
            __builtin_amdgcn_s_sleep(2);
            rel16(ld[16], kb, zo);
            asm volatile("s_waitcnt vmcnt(0)" : "+v"(ld[16]) :: "memory");
          }
#pragma unroll
          for (int j = 0; j < 16; ++j) {
            asm volatile("" : "+v"(ld[j]));
            if (!__all(((ld[j][1] ^ tag32) | (ld[j][3] ^ tag32)) == 0)) {
              unsigned lo = (unsigned)lane * 16;
              asm volatile("" : "+v"(lo));
              rel16(ld[j], kb, lo + j * KVP_B);
            }
          }
          wait_vm<0>();
        }
#pragma unroll
        for (int j = 0; j < 16; ++j)
          s += f32x4{__uint_as_float(ld[j][0] << 16), __uint_as_float(ld[j][0] & 0xffff0000u), __uint_as_float(ld[j][2] << 16), __uint_as_float(ld[j][2] & 0xffff0000u)};
        z = f32x4{__uint_as_float(ld[16][0] << 16), __uint_as_float(ld[16][0] & 0xffff0000u), __uint_as_float(ld[16][2] << 16), __uint_as_float(ld[16][2] & 0xffff0000u)};
      }
      {
#pragma unroll
        for (int i = 0; i < 4; ++i) z[i] = row16_sum(z[i]);
        const s16x4 kme = pk4(s), zme = pk4(z);
        *(LAS s16x8*)(size_t)(spa + 1024 + lane * 16) = cat(kme, zme);
        lds_flag_set(lbase + LDS_FLG + 16 + grp * 4);
        lds_flag_wait(lbase + LDS_FLG + 16 + (grp ^ 1) * 4, 1);
        const s16x8 pa = *(LAS s16x8*)(size_t)(sme + 1024 + lane * 16);
        const s16x4 kpa = __builtin_shufflevector(pa, pa, 0, 1, 2, 3), zpa = __builtin_shufflevector(pa, pa, 4, 5, 6, 7);
        const s16x8 kv8 = rg ? cat(kpa, kme) : cat(kme, kpa), z8 = rg ? cat(zpa, zme) : cat(zme, zpa);
        s16x4 ob[4];
#pragma unroll
        for (int i = 0; i < 4; ++i) {
          const f32x4 num = mfma32(kv8, phq[i], zero4);
          const f32x4 den = mfma32(z8, phq[i], zero4);
          ob[i] = pk4(num * __builtin_amdgcn_rcpf(den[0] + 1e-6f));
        }
#pragma unroll
        for (int p = 0; p < 2; ++p)
          __builtin_amdgcn_raw_buffer_store_b128(__builtin_bit_cast(u32x4, cat(ob[2 * p], ob[2 * p + 1])), rs_hb, rbo(2 * p) + sel * 72 * 1024 + (56 + c) * 1024, 0, 16);
      }
      asm volatile("" ::: "memory");
      b2_tile(4, 0); b2_tile(5, 1);
      wait_vm<8>();
      signal(2);
      lds_flag_wait(lbase + LDS_FLG + 32, 4);
    }

#pragma unroll
    for (int i = 0; i < 4; ++i)
#pragma unroll
      for (int j = 0; j < 4; ++j) acc[i][j] = zero4;
    u32x4 af[NT][4];
    const char* hbp = ws + WS_HB + (size_t)(mt * 4 * 72) * 1024;
    auto load_a = [&](auto TT) {
      constexpr int T = TT;
      const char* p = hbp + kstep_of(T) * 1024;
#pragma unroll
      for (int i = 0; i < 4; ++i) ald16(af[T][i], p + i * 72 * 1024, (unsigned)lane * 16);
    };
    auto comp = [&](auto TT, auto NN) {
      constexpr int T = TT;
      asm volatile("s_waitcnt vmcnt(%4)" : "+v"(af[T][0]), "+v"(af[T][1]), "+v"(af[T][2]), "+v"(af[T][3]) : "n"((int)NN) : "memory");
      s16x8 bf[4];
#pragma unroll
      for (int b = 0; b < 4; ++b) {
        if constexpr (T < NP) bf[b] = b2r[T][b];
        else bf[b] = b2_frag((T - NP) % 5, b);
      }
      if constexpr (ROLE ? true : T >= 8) {
        const int kk = kstep_of(T);
        const bool hh = (ROLE && T >= 2) || kk % 7 == 3;
#pragma unroll
        for (int p = 0; p < 2; ++p) {
          const u32x4 l0 = af[T][2 * p], l1 = af[T][2 * p + 1];
          af[T][2 * p] = u32x4{l0[0], l0[1], hh ? l1[0] : l0[2], hh ? l1[1] : l0[3]};
          af[T][2 * p + 1] = u32x4{hh ? l0[2] : l1[0], hh ? l0[3] : l1[1], l1[2], l1[3]};
        }
      }
#pragma unroll
      for (int i = 0; i < 4; ++i)
#pragma unroll
        for (int b = 0; b < 4; ++b) acc[b][i] = mfma32(bf[b], __builtin_bit_cast(s16x8, af[T][i]), acc[b][i]);
      SB();
    };
    const char* mbp = ws + WS_MB + (size_t)((mt * 8 + nt) * 4 + grp) * 128 + (lane & 7) * 8;
    u64 pv;
    auto check = [&](int kind, auto NN) {
      asm volatile("s_waitcnt vmcnt(%1)" : "+v"(pv) : "n"((int)NN) : "memory");
      if (!__all(pv == tag)) await(kind);
    };
    if constexpr (ROLE == 0) {
#pragma unroll
      for (int T = 4; T < 9; ++T) b2_tile(T, T - 4);
      await(0);
      load_a(IC(0)); load_a(IC(1)); load_a(IC(2)); load_a(IC(3)); load_a(IC(4));
      SB();
      comp(IC(0), IC(16)); load_a(IC(5)); load_a(IC(6));
      comp(IC(1), IC(20)); load_a(IC(7)); ald8(pv, mbp + 32 * 8 * 4 * 128);
      comp(IC(2), IC(21));
      comp(IC(3), IC(17));
      comp(IC(4), IC(13));
      asm volatile("s_waitcnt lgkmcnt(0)" ::: "memory");
      b2_tile(9, 0);
      check(1, IC(4));
      load_a(IC(8)); load_a(IC(9)); load_a(IC(10)); load_a(IC(11));
      comp(IC(5), IC(63));
      asm volatile("s_waitcnt lgkmcnt(0)" ::: "memory");
      b2_tile(10, 1);
      comp(IC(6), IC(63));
      asm volatile("s_waitcnt lgkmcnt(0)" ::: "memory");
      b2_tile(11, 2);
      comp(IC(7), IC(63));
      comp(IC(8), IC(20));
      comp(IC(9), IC(16));
      comp(IC(10), IC(4));
      comp(IC(11), IC(0));
    } else {
      await(1);
      load_a(IC(0)); load_a(IC(1));
      await(2);
      load_a(IC(2)); load_a(IC(3)); load_a(IC(4)); load_a(IC(5));
      comp(IC(0), IC(16)); comp(IC(1), IC(16));
      comp(IC(2), IC(12)); comp(IC(3), IC(8)); comp(IC(4), IC(4)); comp(IC(5), IC(0));
    }
  };
  if (w < 4) role_body(IC(0)); else role_body(IC(1));

  const int om = tid >> 3, oc = tid & 7;
  const size_t goff = (size_t)(mt * 64 + om) * 1024 + nt * 128 + oc * 16;
  const u32x4 xr = *(const u32x4*)(x + goff);
  asm volatile("s_waitcnt lgkmcnt(0)" ::: "memory");
#pragma unroll
  for (int i = 0; i < 4; ++i)
#pragma unroll
    for (int b = 0; b < 4; ++b) lds_wr4(slot_of(w, i) + l15 * 256 + (((4 * b + g) ^ (l15 & 7)) << 4), acc[b][i]);
  lds_bar();
  {
    const int i = w >> 1, m = om & 15;
    const unsigned ro = m * 256;
    const unsigned c0 = ro + (((2 * oc) ^ (m & 7)) << 4), c1 = ro + (((2 * oc + 1) ^ (m & 7)) << 4);
    f32x4 s0 = zero4, s1 = zero4;
#pragma unroll
    for (int ww = 0; ww < 8; ++ww) {
      const unsigned sb = slot_of(ww, i);
      s0 += lds_rd4(sb + c0); s1 += lds_rd4(sb + c1);
    }
    u32x4 o;
    o[0] = pk2(s0[0] + __uint_as_float(xr[0] << 16), s0[1] + __uint_as_float(xr[0] & 0xffff0000u));
    o[1] = pk2(s0[2] + __uint_as_float(xr[1] << 16), s0[3] + __uint_as_float(xr[1] & 0xffff0000u));
    o[2] = pk2(s1[0] + __uint_as_float(xr[2] << 16), s1[1] + __uint_as_float(xr[2] & 0xffff0000u));
    o[3] = pk2(s1[2] + __uint_as_float(xr[3] << 16), s1[3] + __uint_as_float(xr[3] & 0xffff0000u));
    *(u32x4*)(out + goff) = o;
  }
  asm volatile(".Lfk_end:");
  if (bid == 0 && tid == 0) set_flag((u64*)(ws + WS_EPOCH), tag);
}

#if defined(__HIP_DEVICE_COMPILE__)
#pragma clang attribute pop
#endif

extern "C" void kernel_launch(void* const* d_in, const int* in_sizes, int n_in, void* d_out, int out_size, void* d_ws, size_t ws_size,
                              hipStream_t stream) {
  (void)in_sizes; (void)n_in; (void)out_size; (void)ws_size;
  (void)hipFuncSetAttribute((const void*)fused_kernel, hipFuncAttributeMaxDynamicSharedMemorySize, LDS_TOTAL);
  fused_kernel<<<256, 512, LDS_TOTAL, stream>>>((const char*)d_in[0], (const char*)d_in[1], (const char*)d_in[2], (const char*)d_in[3],
                                                (const char*)d_in[5], (const char*)d_in[6], (char*)d_out, (char*)d_ws);
}
```
